```python
import torch

def _softmax(x, axis=-1):
    return torch.softmax(x, dim=axis)
def _gelu(x):
    return torch.nn.functional.gelu(x, approximate="tanh")
def _silu(x):
    return torch.nn.functional.silu(x)
def _sigmoid(x):
    return torch.sigmoid(x)
def _layernorm(x, axis=-1, eps=1e-5):
    m = x.mean(dim=axis, keepdim=True)
    v = x.var(dim=axis, keepdim=True, unbiased=False)
    return (x - m) / torch.sqrt(v + eps)
def _scatter_add(tgt, idx, src, axis=0):
    return tgt.clone().index_add_(axis, idx, src)
def _segment_sum(src, seg, n):
    out = torch.zeros((n,) + src.shape[1:], dtype=src.dtype)
    return out.index_add_(0, seg, src)
def _pad(x, pw):
    flat = tuple(v for pair in reversed(pw) for v in pair)
    return torch.nn.functional.pad(x, flat)

kernel_name = 'sequential_preLN__mha_block__mixer_s_mlp__prompt'

def setup_inputs():

    _w = (lambda a: (lambda t: t.to(torch.bfloat16) if t.is_floating_point() else t)(torch.as_tensor(a)))
    def _A_setup():
        B, S, D, H = 1, 2048, 640, 32
        return {
            "x": _w((lambda *s: torch.randn(*s, dtype=torch.bfloat16) * 0.05)(B, S, D)),
            "wqkv": _w((lambda *s: torch.randn(*s, dtype=torch.bfloat16) * 0.05)(D, 3 * D)),
            "wo": _w((lambda *s: torch.randn(*s, dtype=torch.bfloat16) * 0.05)(D, D)),
        }
    _A_in = _A_setup()

    def _B_setup():
        B, S, D, F = 1, 2048, 640, 1728
        return {
            "x":  _w((lambda *s: torch.randn(*s, dtype=torch.bfloat16) * 0.05)(B, S, D)),
            "w1": _w((lambda *s: torch.randn(*s, dtype=torch.bfloat16) * 3.2)(D, D) * (D ** -0.5)),
            "w2": _w((lambda *s: torch.randn(*s, dtype=torch.bfloat16) * 3.2)(D, D) * (D ** -0.5)),
            "w3": _w((lambda *s: torch.randn(*s, dtype=torch.bfloat16) * 3.2)(D, F) * (D ** -0.5)),
            "w4": _w((lambda *s: torch.randn(*s, dtype=torch.bfloat16) * 3.2)(F, D) * (F ** -0.5)),
        }
    _B_in = _B_setup()

    return {
        "A_x": _A_in["x"],
        "A_wqkv": _A_in["wqkv"],
        "A_wo": _A_in["wo"],
        "B_x": _B_in["x"],
        "B_w1": _B_in["w1"],
        "B_w2": _B_in["w2"],
        "B_w3": _B_in["w3"],
        "B_w4": _B_in["w4"],
    }

def reference(A_x, A_wqkv, A_wo, B_x, B_w1, B_w2, B_w3, B_w4):
    def _A_ref(x, wqkv, wo):
        B, S, D = x.shape
        H = 32
        Dh = D // H
        qkv = (lambda x, *d: x.permute(*d))((x @ wqkv).reshape(B, S, 3, H, Dh), 2, 0, 3, 1, 4)
        q, k, v = qkv[0], qkv[1], qkv[2]
        scores = torch.einsum("bhqd,bhkd->bhqk", q, k) * (Dh ** -0.5)
        p = _softmax(scores, axis=-1)
        o = torch.einsum("bhqk,bhkd->bhqd", p, v)
        return (lambda x, *d: x.permute(*d))(o, 0, 2, 1, 3).reshape(B, S, D) @ wo

    def _B_ref(x, w1, w2, w3, w4):
        B, S, D = x.shape
        h = _gelu(_layernorm(x, axis=-1) @ w1)
        c = D // 4
        g0 = _pad(h[:, :, 0*c:1*c], ((0, 0), (1, 0), (0, 0)))[:, :S, :]
        g1 = _pad(h[:, :, 1*c:2*c], ((0, 0), (0, 1), (0, 0)))[:, 1:, :]
        g2 = _pad(h[:, :, 2*c:3*c], ((0, 0), (2, 0), (0, 0)))[:, :S, :]
        g3 = _pad(h[:, :, 3*c:   ], ((0, 0), (0, 2), (0, 0)))[:, 2:, :]
        hs = torch.concatenate([g0, g1, g2, g3], axis=-1) @ w2
        x1 = x + hs
        h2 = _gelu(_layernorm(x1, axis=-1) @ w3) @ w4
        return x1 + h2

    x = A_x
    h = x + _A_ref(_layernorm(x, axis=-1), A_wqkv, A_wo)
    h2 = h + _B_ref(_layernorm(h, axis=-1), B_w1, B_w2, B_w3, B_w4)
    return h2
```

```cpp
#include <hip/hip_runtime.h>
#include <hip/hip_bf16.h>
typedef __hip_bfloat16 bf16;
constexpr int S=2048, D=640, H=32, DH=20, F=1728;

__device__ inline float b2f(bf16 x){return __bfloat162float(x);}
__device__ inline float gelu_t(float x){ float u=0.7978845608028654f*(x+0.044715f*x*x*x); return 0.5f*x*(1.f+tanhf(u)); }

__global__ void ln_f(const float* in, float* out){
  int r=blockIdx.x; __shared__ float red[256];
  float s=0; for(int i=threadIdx.x;i<D;i+=256) s+=in[r*D+i];
  red[threadIdx.x]=s; __syncthreads();
  for(int o=128;o>0;o>>=1){ if(threadIdx.x<o) red[threadIdx.x]+=red[threadIdx.x+o]; __syncthreads(); }
  float m=red[0]/D; __syncthreads();
  float v=0; for(int i=threadIdx.x;i<D;i+=256){ float d=in[r*D+i]-m; v+=d*d; }
  red[threadIdx.x]=v; __syncthreads();
  for(int o=128;o>0;o>>=1){ if(threadIdx.x<o) red[threadIdx.x]+=red[threadIdx.x+o]; __syncthreads(); }
  float rs=rsqrtf(red[0]/D+1e-5f);
  for(int i=threadIdx.x;i<D;i+=256) out[r*D+i]=(in[r*D+i]-m)*rs;
}
__global__ void cvt(const bf16* in, float* out, int n){ int i=blockIdx.x*256+threadIdx.x; if(i<n) out[i]=b2f(in[i]); }
__global__ void gemm(const float* A, const bf16* W, float* C, int M,int N,int K,int act,const float* add1,const float* add2, bf16* outb){
  __shared__ float As[16][17], Ws[16][17];
  int tx=threadIdx.x&15, ty=threadIdx.x>>4; int r=blockIdx.y*16+ty, c=blockIdx.x*16+tx; float acc=0;
  for(int k0=0;k0<K;k0+=16){ As[ty][tx]=A[r*K+k0+tx]; Ws[ty][tx]=b2f(W[(k0+ty)*N+c]); __syncthreads();
    for(int k=0;k<16;k++) acc+=As[ty][k]*Ws[k][tx]; __syncthreads(); }
  if(act) acc=gelu_t(acc);
  if(add1) acc+=add1[r*N+c]; if(add2) acc+=add2[r*N+c];
  if(outb) outb[r*N+c]=__float2bfloat16(acc); else C[r*N+c]=acc;
}
__global__ void attn(const float* qkv, float* o){
  int h=blockIdx.y; int q=blockIdx.x*64+threadIdx.x;
  float qv[DH]; for(int d=0;d<DH;d++) qv[d]=qkv[q*3*D+h*DH+d]*0.22360679774997896f;
  float m=-1e30f,l=0,acc[DH]; for(int d=0;d<DH;d++) acc[d]=0;
  for(int k=0;k<S;k++){ float s=0; for(int d=0;d<DH;d++) s+=qv[d]*qkv[k*3*D+D+h*DH+d];
    float mn=fmaxf(m,s); float a=__expf(m-mn), p=__expf(s-mn); l=l*a+p; for(int d=0;d<DH;d++) acc[d]=acc[d]*a+p*qkv[k*3*D+2*D+h*DH+d]; m=mn; }
  for(int d=0;d<DH;d++) o[q*D+h*DH+d]=acc[d]/l;
}
__global__ void shiftk(const float* in, float* out){
  int i=blockIdx.x*256+threadIdx.x; if(i>=S*D) return; int r=i/D,c=i%D; int g=c/160; int sh=(g==0)?-1:(g==1)?1:(g==2)?-2:2; int rr=r+sh;
  out[i]=(rr>=0&&rr<S)?in[rr*D+c]:0.f;
}
extern "C" void kernel_launch(void* const* d_in, const int* in_sizes, int n_in, void* d_out, int out_size, void* d_ws, size_t ws_size, hipStream_t st){
  const bf16* x=(const bf16*)d_in[0]; const bf16* wqkv=(const bf16*)d_in[1]; const bf16* wo=(const bf16*)d_in[2];
  const bf16* w1=(const bf16*)d_in[4]; const bf16* w2=(const bf16*)d_in[5]; const bf16* w3=(const bf16*)d_in[6]; const bf16* w4=(const bf16*)d_in[7];
  float* p=(float*)d_ws; float* xf=p; p+=S*D; float* ln1=p; p+=S*D; float* qkv=p; p+=S*3*D; float* o=p; p+=S*D; float* h=p; p+=S*D;
  float* y=p; p+=S*D; float* y2=p; p+=S*D; float* hm=p; p+=S*D; float* hs=p; p+=S*D; float* x1=p; p+=S*D; float* l3=p; p+=S*D; float* g=p; p+=S*F;
  cvt<<<S*D/256,256,0,st>>>(x,xf,S*D);
  ln_f<<<S,256,0,st>>>(xf,ln1);
  gemm<<<dim3(3*D/16,S/16),256,0,st>>>(ln1,wqkv,qkv,S,3*D,D,0,nullptr,nullptr,nullptr);
  attn<<<dim3(S/64,H),64,0,st>>>(qkv,o);
  gemm<<<dim3(D/16,S/16),256,0,st>>>(o,wo,h,S,D,D,0,xf,nullptr,nullptr);
  ln_f<<<S,256,0,st>>>(h,y);
  ln_f<<<S,256,0,st>>>(y,y2);
  gemm<<<dim3(D/16,S/16),256,0,st>>>(y2,w1,hm,S,D,D,1,nullptr,nullptr,nullptr);
  shiftk<<<S*D/256,256,0,st>>>(hm,hs);
  gemm<<<dim3(D/16,S/16),256,0,st>>>(hs,w2,x1,S,D,D,0,y,nullptr,nullptr);
  ln_f<<<S,256,0,st>>>(x1,l3);
  gemm<<<dim3(F/16,S/16),256,0,st>>>(l3,w3,g,S,F,D,1,nullptr,nullptr,nullptr);
  gemm<<<dim3(D/16,S/16),256,0,st>>>(g,w4,nullptr,S,D,F,0,x1,h,(bf16*)d_out);
}
```

```cpp
#include <hip/hip_runtime.h>
#include <stdint.h>

typedef __bf16 bf16x8 __attribute__((ext_vector_type(8)));
typedef __bf16 bf16x2 __attribute__((ext_vector_type(2)));
typedef short s16x4 __attribute__((ext_vector_type(4)));
typedef float f32x16 __attribute__((ext_vector_type(16)));
typedef float f32x4 __attribute__((ext_vector_type(4)));
typedef unsigned u32x4 __attribute__((ext_vector_type(4)));
typedef unsigned u32x2 __attribute__((ext_vector_type(2)));
#define LAS __attribute__((address_space(3)))
#define DI __device__ __forceinline__

constexpr int S = 2048, D = 640, F = 1728;
constexpr int QIMG_OFF = 0;
constexpr int QK_B = 64 * 80 * 512;
constexpr int KIMG_OFF = QIMG_OFF + QK_B;
constexpr int V_TILE = 1344;
constexpr int V_ROW = 648 * 16;
constexpr int VIMG_OFF = KIMG_OFF + QK_B;
constexpr int VIMG_B = 64 * 4 * V_ROW;
constexpr int OIMG_OFF = VIMG_OFF + VIMG_B;
constexpr int OIMG_B = 128 * 20 * 1024;
constexpr int HIMG_OFF = OIMG_OFF + OIMG_B;
constexpr int MIMG_OFF = HIMG_OFF + OIMG_B;
constexpr int XIMG_OFF = MIMG_OFF + OIMG_B;
constexpr int GIMG_OFF = XIMG_OFF + OIMG_B;
constexpr int GIMG_B = 128 * 54 * 1024;
constexpr int ST_OFF = GIMG_OFF + GIMG_B;
constexpr int ST_B = 128 * 8 * 128;
constexpr int CNT_OFF = ST_OFF + 2 * ST_B;
constexpr int CNT_O = 4 * 128, CNT_L = 5 * 128, CNT_W = 6 * 128;
constexpr int CNT_B = 800 * 128;
constexpr int WT_OFF = CNT_OFF + CNT_B;
constexpr int WT_SQ = 800 * 1024;
constexpr int WT3_OFF = WT_OFF + 3 * WT_SQ;
constexpr int WT4_OFF = WT3_OFF + 8 * 280 * 1024;
constexpr int CS1_OFF = WT4_OFF + 8 * 270 * 1024;
constexpr int CS3_OFF = CS1_OFF + 40 * 20 * 64;
constexpr int WS_END = CS3_OFF + 108 * 20 * 64;
constexpr int KV_CHUNK = 8 * (1024 + 512 + V_TILE);

DI unsigned pk2(float a, float b) { bf16x2 p; p[0] = (__bf16)a; p[1] = (__bf16)b; return __builtin_bit_cast(unsigned, p); }
DI f32x16 mfma32(u32x4 a, u32x4 b, f32x16 c) { return __builtin_amdgcn_mfma_f32_32x32x16_bf16(__builtin_bit_cast(bf16x8, a), __builtin_bit_cast(bf16x8, b), c, 0, 0, 0); }
DI f32x4 mfma16(u32x4 a, u32x4 b, f32x4 c) { return __builtin_amdgcn_mfma_f32_16x16x32_bf16(__builtin_bit_cast(bf16x8, a), __builtin_bit_cast(bf16x8, b), c, 0, 0, 0); }
DI f32x16 mfma32k8(u32x2 a, u32x2 b, f32x16 c) { return __builtin_amdgcn_mfma_f32_32x32x8bf16_1k(__builtin_bit_cast(s16x4, a), __builtin_bit_cast(s16x4, b), c, 0, 0, 0); }
DI u32x2 trread(LAS const char* p) { return __builtin_bit_cast(u32x2, __builtin_amdgcn_ds_read_tr16_b64_v4i16((LAS s16x4*)p)); }
DI float swap32_other(float v) {
  auto r = __builtin_amdgcn_permlane32_swap(__float_as_uint(v), __float_as_uint(v), false, false);
  return __uint_as_float((threadIdx.x & 32) ? r[0] : r[1]);
}
DI void dma16(__amdgpu_buffer_rsrc_t rs, unsigned ldsaddr, int voff, int soff) {
  asm volatile("s_mov_b32 m0, %0\n\ts_nop 0\n\tbuffer_load_dwordx4 %1, %2, %3 offen lds" ::"s"(ldsaddr), "v"(voff), "s"(rs), "s"(soff) : "memory");
}
template <int N> DI void waitv() { asm volatile("s_waitcnt vmcnt(%0)" ::"i"(N) : "memory"); }
#define BAR() asm volatile("s_waitcnt lgkmcnt(0)\n\ts_barrier" ::: "memory")
#define RAWBAR() asm volatile("s_barrier" ::: "memory")
#define SB() __builtin_amdgcn_sched_barrier(0)
#define PIN(x) asm volatile("" : "+v"(x))

DI void st16(char* p, u32x4 v) { asm volatile("global_store_dwordx4 %0, %1, off sc1\n\ts_nop 1" ::"v"(p), "v"(v) : "memory"); }

#define CODE_ADDR(out, sym) asm volatile("s_getpc_b64 s[20:21]\n\ts_add_u32 s20, s20, " sym "@rel32@lo+4\n\ts_addc_u32 s21, s21, " sym "@rel32@hi+12\n\ts_mov_b64 %0, s[20:21]" : "=s"(out) :: "s20", "s21", "scc")
DI unsigned code_touch(const char* a, const char* b, int part, int nparts) {
  const int lines = b > a ? (int)((b - a) >> 6) + 1 : 1, per = (lines + nparts - 1) / nparts;
  const int l = threadIdx.x & 63;
  return *(const unsigned*)(a + min(part * per + min(l, per - 1), lines - 1) * 64);
}
struct WPtrs { const char* wo; const char* w1; const char* w2; const char* w3; const char* w4; };
constexpr int QKV_PRE = 2, QKV_DEPTH = 4;
DI void qkv_loader(const char* x, const char* w, int rb, int ct, int wv, unsigned lds0, int rot) {
  const int l = threadIdx.x & 63;
  auto rx = __builtin_amdgcn_make_buffer_rsrc((void*)x, 0, S * D * 2, 0x00020000);
  auto rw = __builtin_amdgcn_make_buffer_rsrc((void*)w, 0, D * 3 * D * 2, 0x00020000);
  const int va0 = (l >> 3) * 1280 + (((l & 7) ^ ((l >> 4) & 7)) << 4);
  const int va1 = (l >> 3) * 1280 + (((l & 7) ^ ((4 + (l >> 4)) & 7)) << 4);
  const int vb0 = (l >> 4) * 3840 + (((l & 15) ^ ((l >> 4) << 2)) << 4), vb1 = vb0 ^ 32;
  const int sa = (rb * 128 + wv * 32) * 1280, sb = (wv * 16) * 3840 + ct * 256;
  auto issue = [&](int ci) {
    const unsigned la = lds0 + (ci % 5) * 32768 + wv * 4096, lb = la + 16384;
    int kc = ci + rot; kc = kc >= 10 ? kc - 10 : kc;
#pragma unroll
    for (int i = 0; i < 4; ++i) {
      dma16(rx, la + i * 1024, (i & 1) ? va1 : va0, sa + i * 8 * 1280 + kc * 128);
      dma16(rw, lb + i * 1024, (i & 2) ? vb1 : vb0, sb + (kc * 64 + i * 4) * 3840);
    }
  };
#pragma unroll
  for (int c = 0; c < QKV_PRE; ++c) issue(c);
  int issued = QKV_PRE;
#pragma unroll
  for (int c = 0; c < 10; ++c) {
    const int out = issued - c - 1;
    if (out == 0) waitv<0>(); else if (out == 1) waitv<8>(); else if (out == 2) waitv<16>(); else if (out == 3) waitv<24>(); else waitv<32>();
    asm volatile("s_barrier" ::: "memory");
    const int lim = c + QKV_DEPTH < 10 ? c + QKV_DEPTH : 10;
    if (issued < lim) { issue(issued); ++issued; }
    if (issued < lim) { issue(issued); ++issued; }
  }
}

template <bool ISV>
DI void qkv_body(char* ws, int rb, int ct, int wv, LAS char* lds, int j) {
  const int l = threadIdx.x & 63, wm = wv >> 2, wn = wv & 3;
  const int m = l & 15, q = l >> 4;
  LAS const char* xa[3][2][2];
  LAS const char* wa[3][2];
  {
    const int r4 = m >> 2, p = l & 3;
#pragma unroll
    for (int w = 0; w < 3; ++w) {
#pragma unroll
      for (int i = 0; i < 2; ++i) {
#pragma unroll
        for (int s = 0; s < 2; ++s) {
          xa[w][s][i] = lds + w * 65536 + (wm * 64 + (i ^ wn) * 16 + m) * 128 + (((4 * s + q) ^ ((m >> 1) & 7)) << 4);
          PIN(xa[w][s][i]);
        }
        const int chunk = wn * 4 + (i ^ wm) * 2 + (p >> 1);
        wa[w][i] = lds + w * 65536 + 16384 + (8 * q + r4) * 256 + ((chunk ^ (r4 << 2) ^ ((q & 1) << 1)) << 4) + (p & 1) * 8;
        PIN(wa[w][i]);
      }
    }
  }
  const int xo = (wn & 2) ? -4096 : 4096;
  f32x4 acc[4][2], gram = f32x4{}, rsum = f32x4{}, csum = f32x4{};
#pragma unroll
  for (int i = 0; i < 4; ++i) { acc[i][0] = f32x4{}; acc[i][1] = f32x4{}; }
  const unsigned ONES = 0x3f803f80u;
  const unsigned selv = m == 0 ? ONES : 0u;
  const u32x4 sel = {selv, selv, selv, selv};
  u32x4 xf[2][4], wf[2][2];
  LAS const char* xb[3][2][2];
#pragma unroll
  for (int w = 0; w < 3; ++w)
#pragma unroll
    for (int s = 0; s < 2; ++s)
#pragma unroll
      for (int i = 0; i < 2; ++i) { xb[w][s][i] = xa[w][s][i] + xo; PIN(xb[w][s][i]); }
  auto rd = [&](int st, int i) {
    const int off = ((st >> 1) % 5) * 32768, w = off >> 16, o = off & 65535, b = st & 1;
    if (i < 4) {
      const u32x2 v = trread(wa[w][i >> 1] + (o + b * 8192 + (i & 1) * 1024));
      wf[b][i >> 1][2 * (i & 1)] = v[0]; wf[b][i >> 1][2 * (i & 1) + 1] = v[1];
    } else xf[b][i - 4] = *(LAS const u32x4*)((i < 6 ? xa[w][b][i & 1] : xb[w][b][i & 1]) + o);
  };
  BAR();
  unsigned pf;
  { const char *c0, *c1; CODE_ADDR(c0, "fused_kernel"); CODE_ADDR(c1, "code_end"); pf = code_touch(c0, c1, min(j * 8 + wv, 223), 224); }
#pragma unroll
  for (int i = 0; i < 8; ++i) rd(0, i);
#pragma unroll
  for (int st = 0; st < 20; ++st) {
    const int b = st & 1;
    if (b && st < 19) BAR();
    SB();
#pragma unroll
    for (int i = 0; i < 11; ++i) {
      if (i < 8) {
        const int mt = 3 - (i >> 1), nt = 1 - (i & 1);
        acc[mt][nt] = ISV ? mfma16(xf[b][mt], wf[b][nt], acc[mt][nt]) : mfma16(wf[b][nt], xf[b][mt], acc[mt][nt]);
      } else if (i == 8) gram = mfma16(xf[b][0], xf[b][0], gram);
      else if (i == 9) rsum = mfma16(sel, xf[b][0], rsum);
      else csum = mfma16(sel, wf[b][0], csum);
      SB();
      if (st < 19 && i < 8) { rd(st + 1, i); SB(); }
    }
  }
  asm volatile("" :: "v"(pf));
  LAS float* st_sum = (LAS float*)lds;
  LAS float* st_sq = st_sum + 128;
  LAS float* st_cs = st_sum + 256;
  {
    const int row = wm * 64 + wn * 16 + m;
    const float d = (l & 2) ? ((l & 1) ? gram[3] : gram[2]) : ((l & 1) ? gram[1] : gram[0]);
    if ((m >> 2) == q) st_sq[row] = d;
    if (q == 0) { st_sum[row] = rsum[0]; st_cs[wn * 32 + wm * 16 + m] = csum[0]; }
  }
  BAR();
  if (!ISV) {
    const bool isq = ct < 5;
    const float sc = isq ? 0.22360679774997896f * 1.4426950408889634f : 1.f;
    char* base = ws + (isq ? QIMG_OFF : KIMG_OFF) + ((isq ? ct : ct - 5) * 16 + wn * 4 + 2 * ((q & 1) ^ wm) + (q >> 1)) * 512 + m * 16;
    f32x4 cs[2];
#pragma unroll
    for (int nt = 0; nt < 2; ++nt) cs[nt] = *(LAS const f32x4*)(st_cs + wn * 32 + (nt ^ wm) * 16 + 4 * q);
#pragma unroll
    for (int mt = 0; mt < 4; ++mt) {
      const int rt = wm * 4 + (mt ^ wn);
      const float mean = st_sum[rt * 16 + m] * (1.f / D);
      const float rs = __builtin_amdgcn_rsqf(fmaxf(st_sq[rt * 16 + m] * (1.f / D) - mean * mean, 0.f) + 1e-5f) * sc;
      const f32x4 va = (acc[mt][0] - mean * cs[0]) * rs, vb = (acc[mt][1] - mean * cs[1]) * rs;
      auto r0 = __builtin_amdgcn_permlane16_swap(pk2(va[0], va[1]), pk2(vb[0], vb[1]), false, false);
      auto r1 = __builtin_amdgcn_permlane16_swap(pk2(va[2], va[3]), pk2(vb[2], vb[3]), false, false);
      st16(base + (rb * 4 + (rt >> 1)) * 40960 + (rt & 1) * 256, u32x4{r0[0], r1[0], r0[1], r1[1]});
    }
  } else {
    float cs[2];
#pragma unroll
    for (int nt = 0; nt < 2; ++nt) cs[nt] = st_cs[wn * 32 + (nt ^ wm) * 16 + m];
    char* base = ws + VIMG_OFF + (q & 1) * V_ROW + ((ct - 10) * 128 + wn * 32 + ((q >> 1) ^ wm) * 16 + m) * 16;
#pragma unroll
    for (int mt = 0; mt < 4; ++mt) {
      const int rt = wm * 4 + (mt ^ wn);
      const f32x4 mean = *(LAS const f32x4*)(st_sum + rt * 16 + 4 * q) * (1.f / D);
      const f32x4 var = *(LAS const f32x4*)(st_sq + rt * 16 + 4 * q) * (1.f / D) - mean * mean;
      f32x4 rs;
#pragma unroll
      for (int i = 0; i < 4; ++i) rs[i] = __builtin_amdgcn_rsqf(fmaxf(var[i], 0.f) + 1e-5f);
      const f32x4 va = (acc[mt][0] - mean * cs[0]) * rs, vb = (acc[mt][1] - mean * cs[1]) * rs;
      auto r0 = __builtin_amdgcn_permlane32_swap(pk2(va[0], va[1]), pk2(vb[0], vb[1]), false, false);
      auto r1 = __builtin_amdgcn_permlane32_swap(pk2(va[2], va[3]), pk2(vb[2], vb[3]), false, false);
      st16(base + (rb * 8 + rt) * 2 * V_ROW, u32x4{r0[0], r1[0], r0[1], r1[1]});
    }
    if (ct == 14 && wv < 4 && l < 32) st16(ws + VIMG_OFF + ((rb * 4 + wv) * 4 + (l >> 3)) * V_ROW + 10240 + (l & 7) * 16, u32x4{ONES, ONES, ONES, ONES});
  }
}

struct QkvArgs { const char* x; const char* w; char* ws; };
extern "C" __global__ void __launch_bounds__(768, 3) qkv_kernel(QkvArgs args) {
  const char* const x = args.x;
  const char* const w = args.w;
  char* const ws = args.ws;
  __shared__ __attribute__((aligned(16))) char lds[163840];
  asm volatile("" :: "s"(x), "s"(w), "s"(ws));
  const int xc = blockIdx.x & 7, j = blockIdx.x >> 3;
  const int rb = (xc >> 1) * 4 + (j & 3), ct = (xc & 1) * 8 + (j >> 2);
  const int wv = __builtin_amdgcn_readfirstlane(threadIdx.x >> 6);
  if (__builtin_expect(wv >= 8 && ct < 15, 1)) {
    qkv_loader(x, w, rb, ct, wv - 8, (unsigned)(uintptr_t)(LAS char*)lds, ((j >> 2) + 3 * (j & 3)) % 10);
    return;
  }
  if (ct >= 15) {
    if (threadIdx.x < 400) *(u32x4*)(ws + CNT_OFF + (rb * 400 + threadIdx.x) * 16) = u32x4{0u, 0u, 0u, 0u};
    return;
  }
  unsigned pf;
  { const char *c0, *c1; CODE_ADDR(c0, "qkv_kernel"); CODE_ADDR(c1, "fused_kernel"); pf = code_touch(c0, c1, min(j * 8 + wv, 223), 224); }
  if (ct >= 10) qkv_body<true>(ws, rb, ct, wv, (LAS char*)lds, j);
  else qkv_body<false>(ws, rb, ct, wv, (LAS char*)lds, j);
  asm volatile("" :: "v"(pf));
}

#define GAS __attribute__((address_space(1)))
DI void signal(char* ws, int off, bool lane) {
  waitv<0>();
  if (lane) __hip_atomic_fetch_add((GAS unsigned*)(ws + off), 1u, __ATOMIC_RELAXED, __HIP_MEMORY_SCOPE_AGENT);
}
DI void wait_ge(char* ws, int off, unsigned target) {
  GAS unsigned* p = (GAS unsigned*)(ws + off);
  unsigned spins = 0;
  while (!__all(__hip_atomic_load(p, __ATOMIC_RELAXED, __HIP_MEMORY_SCOPE_AGENT) >= target))
    if (++spins > (1u << 21)) break;
  asm volatile("" ::: "memory");
}

DI float max16(const f32x16& s) {
  float a = fmaxf(fmaxf(s[0], s[1]), s[2]), b = fmaxf(fmaxf(s[3], s[4]), s[5]);
  a = fmaxf(fmaxf(a, s[6]), s[7]); b = fmaxf(fmaxf(b, s[8]), s[9]);
  a = fmaxf(fmaxf(a, s[10]), s[11]); b = fmaxf(fmaxf(b, s[12]), s[13]);
  a = fmaxf(fmaxf(a, s[14]), s[15]);
  return fmaxf(a, b);
}
#define EX2(x) __builtin_amdgcn_exp2f(x)
DI void exp_pack(const f32x16& s, u32x4& p0, u32x4& p1) {
  float e[16];
#pragma unroll
  for (int i = 0; i < 16; ++i) e[i] = EX2(s[i]);
  p0 = u32x4{pk2(e[0], e[1]), pk2(e[2], e[3]), pk2(e[4], e[5]), pk2(e[6], e[7])};
  p1 = u32x4{pk2(e[8], e[9]), pk2(e[10], e[11]), pk2(e[12], e[13]), pk2(e[14], e[15])};
}

DI void attn_finish(char* ws, f32x16 o, u32x4 q0, u32x2 q1, int qt, int head, int l) {
  const int h = l >> 5, r32 = l & 31, u = head >> 1, e = head & 1;
  const int pa = (5 * u + 3 * e) * 512, pm = (5 * u + 2) * 512;
  float lsum = o[8];
  { const float t = swap32_other(lsum); lsum = h ? lsum : t; }
  bool ok = lsum > 1e-18f && lsum < 1e18f;
#pragma unroll
  for (int i = 0; i < 8; ++i) ok = ok && fabsf(o[i]) < 3e38f;
#pragma unroll
  for (int i = 8; i < 12; ++i) ok = ok && (h || fabsf(o[i]) < 3e38f);
  if (__builtin_expect(!__all(ok), 0)) {
    if (h) q1 = u32x2{0u, 0u};
    const int vc = r32 < 20 ? head * 20 + r32 : 640;
    float m = -INFINITY;
    o = f32x16{};
#pragma unroll 1
    for (int t = 0; t < 64; ++t) {
      const char* kt = ws + KIMG_OFF + t * 40960;
      const char* vt = ws + VIMG_OFF + (t * 4 + h) * V_ROW + vc * 16;
      const u32x4 fk0 = *(const u32x4*)(kt + pa + l * 16);
      const u32x2 fk1 = *(const u32x2*)(kt + pm + r32 * 16 + e * 8);
      const u32x4 fv0 = *(const u32x4*)(vt), fv1 = *(const u32x4*)(vt + 2 * V_ROW);
      f32x16 s = mfma32k8(fk1, q1, f32x16{});
      s = mfma32(fk0, q0, s);
      float mt = max16(s);
      mt = fmaxf(mt, swap32_other(mt));
      const float mn = fmaxf(m, mt);
      const float al = EX2(m - mn);
      m = mn;
#pragma unroll
      for (int i = 0; i < 16; ++i) { s[i] -= mn; o[i] *= al; }
      u32x4 f0, f1;
      exp_pack(s, f0, f1);
      o = mfma32(fv0, f0, o);
      o = mfma32(fv1, f1, o);
    }
    lsum = o[8];
    const float t = swap32_other(lsum); lsum = h ? lsum : t;
  }
  const float inv = 1.f / lsum;
  const int row = qt * 32 + r32;
  auto rs = __builtin_amdgcn_make_buffer_rsrc((void*)ws, 0, WS_END, 0x00020000);
  const int ob = OIMG_OFF + (row >> 4) * 20480;
  __builtin_amdgcn_raw_buffer_store_b128(u32x4{pk2(o[0] * inv, o[1] * inv), pk2(o[2] * inv, o[3] * inv), pk2(o[4] * inv, o[5] * inv), pk2(o[6] * inv, o[7] * inv)},
                                         rs, ob + u * 1024 + (2 * e + h) * 256 + (row & 15) * 16, 0, 16);
  if (!h) __builtin_amdgcn_raw_buffer_store_b64(u32x2{pk2(o[8] * inv, o[9] * inv), pk2(o[10] * inv, o[11] * inv)}, rs, ob + 16384 + head * 128 + (row & 15) * 8, 0, 16);
  signal(ws, CNT_OFF + (CNT_O + qt * 2 + (l & 1)) * 128, l < 2);
}


struct Job { const char* src; int bytes, pitch, o0, o1, kind, dst, cs, dup; };
DI Job retile_job(const WPtrs& wp, int f) {
  Job J;
  int tile, s, k0, k1;
  J.cs = 0; J.kind = 0; J.dup = 0; J.pitch = D * 2;
  if (f < 2400) {
    const int m = f / 800, r = f - m * 800, tg = r / 80;
    tile = tg * 4 + (r & 3); s = (r - tg * 80) >> 2;
    J.src = m == 0 ? wp.wo : m == 1 ? wp.w1 : wp.w2; J.bytes = D * D * 2;
    J.dst = WT_OFF + m * WT_SQ + ((tile / 5) * 100 + s * 5 + tile % 5) * 1024;
    if (m == 1) J.cs = CS1_OFF + (tile * 20 + s) * 64;
    if (m == 0) {
      if (s < 16) { k0 = 40 * s; k1 = k0 + 8; J.kind = 2; } else { k0 = 160 * (s - 16) + 16; k1 = k0 + 80; J.kind = 3; }
    } else if (s < 16) { k0 = 80 * (s >> 1) + 32 * (s & 1); k1 = k0 + 16; }
    else { k0 = 160 * (s - 16) + 64; k1 = k0 + 80; }
  } else if (f < 4560) {
    const int r = f - 2400, tg = r / 80;
    tile = tg * 4 + (r & 3); s = (r - tg * 80) >> 2;
    J.src = wp.w3; J.bytes = D * F * 2; J.pitch = F * 2; J.kind = 1;
    const int sl = tile < 84 ? tile / 7 : 12 + (tile - 84) / 6, t = tile < 84 ? tile - sl * 7 : tile - 84 - (sl - 12) * 6;
    J.dst = WT3_OFF + (sl * 140 + (t < 4 ? s * 4 + t : 80 + s * 3 + t - 4)) * 1024;
    J.dup = sl >= 12 && t == 5;
    J.cs = CS3_OFF + (tile * 20 + s) * 64;
    if (s < 16) { k0 = 80 * (s >> 1) + 32 * (s & 1); k1 = k0 + 16; }
    else { k0 = 160 * (s - 16) + 64; k1 = k0 + 80; }
  } else {
    const int r = f - 4560, tg = r / 216;
    tile = tg * 4 + (r & 3); s = (r - tg * 216) >> 2;
    J.src = wp.w4; J.bytes = D * F * 2;
    J.dst = WT4_OFF + ((tile / 5) * 270 + s * 5 + tile % 5) * 1024;
    if (s < 48) {
      const int sc = s < 32 ? s >> 1 : s - 32, tp = s < 32 ? s & 1 : 2;
      k0 = (sc < 12 ? sc * 112 : 1344 + (sc - 12) * 96) + tp * 32; k1 = k0 + 16;
    } else { k0 = (s - 48) * 224 + 96; k1 = k0 + 112; }
  }
  J.o0 = k0 * J.pitch + tile * 32; J.o1 = k1 * J.pitch + tile * 32;
  return J;
}

constexpr int SLOT = 30720, RING = 5 * SLOT, CSL_OFF = RING;
constexpr int CZ_OFF = 155648, CZ_B = 6144, CK_OFF = CZ_OFF + CZ_B, CK_B = 2048;
constexpr int MERGE_OFF = 3 * KV_CHUNK - 24576;
constexpr int slot_of(int g) { return (g + 3) % 5; }
constexpr int CHF = 28, NCH = 27;
constexpr int ch_cnt(int g) { return g < 12 ? ((g & 3) == 3 ? 4 : 7) : g == NCH - 1 ? 5 : 7; }
constexpr bool ch_first(int g) { return g == 0 || g == 4 || g == 8 || g == 12 || g == 17; }
constexpr int W3E = 14;
constexpr int ch_lim(int g) {
  int n = 0;
  if (ch_first(g)) n = g + 5;
  else if (g < 12) n = g + 4;
  else if (g < W3E) n = 17;
  else n = g + 4;
  return n < NCH ? n : NCH;
}
constexpr int ch_lo(int g) { return g == 0 ? 3 : ch_lim(g - 1); }
constexpr int ch_out(int g) {
  if (g < 3) return 63;
  int n = 0;
  bool seen = false;
  for (int b = 0; b < g; ++b)
    for (int c = ch_lo(b); c < ch_lim(b); ++c) { if (seen) n += ch_cnt(c); if (c == g) seen = true; }
  return n;
}
struct LoaderCtx { __amdgpu_buffer_rsrc_t rs; unsigned lds0; int sq, s3, s4, vl; };
#define DMA_HEAD "s_mov_b32 m0, %0\n\ts_nop 0\n\tbuffer_load_dwordx4 %1, %2, %3 offen sc1 lds"
#define DMA_MORE(o) "\n\tbuffer_load_dwordx4 %1, %2, %3 offen offset:" #o " sc1 lds"
template <int N> DI void dma_run(__amdgpu_buffer_rsrc_t rs, unsigned ldsaddr, int voff, int soff) {
  static_assert(N == 1 || N == 3 || N == 4, "");
  if (N == 1) asm volatile(DMA_HEAD ::"s"(ldsaddr), "v"(voff), "s"(rs), "s"(soff) : "memory");
  if (N == 3) asm volatile(DMA_HEAD DMA_MORE(1024) DMA_MORE(2048) ::"s"(ldsaddr), "v"(voff), "s"(rs), "s"(soff) : "memory");
  if (N == 4) asm volatile(DMA_HEAD DMA_MORE(1024) DMA_MORE(2048) DMA_MORE(3072) ::"s"(ldsaddr), "v"(voff), "s"(rs), "s"(soff) : "memory");
}
template <int G> DI void chain_issue(const LoaderCtx& c, int lw) {
  constexpr int n = ch_cnt(G);
  const int src = (G < 12 ? c.sq + (G / 4) * WT_SQ + (G % 4) * CHF * 1024 : G < 17 ? c.s3 + (G - 12) * CHF * 1024 : c.s4 + (G - 17) * CHF * 1024) + lw * n * 1024;
  const unsigned dst = c.lds0 + slot_of(G) * SLOT + lw * n * 1024;
  dma_run<4>(c.rs, dst, c.vl, src);
  if constexpr (n > 4) dma_run<n - 4>(c.rs, dst + 4096, c.vl, src + 4096);
}
DI void chain_colsum(__amdgpu_buffer_rsrc_t rs, LAS float* csl, int cb, int sl) {
  const int c = threadIdx.x & 255, t3 = sl < 12 ? sl * 7 : 84 + (sl - 12) * 6;
  const int p = c < 80 ? CS1_OFF + (cb * 5 + (c >> 4)) * 1280 + (c & 15) * 4 : CS3_OFF + min(t3 + ((c - 80) >> 4), 107) * 1280 + (c & 15) * 4;
  float a = 0.f;
#pragma unroll
  for (int s = 0; s < 20; ++s) a += __uint_as_float(__builtin_amdgcn_raw_buffer_load_b32(rs, p + s * 64, 0, 16));
  if (c < 192) csl[c] = a;
}
template <int G> DI void chain_load_step(const LoaderCtx& c, int lw) {
  waitv<ch_out(G)>();
  RAWBAR();
  constexpr int lo = ch_lo(G), hi = ch_lim(G);
  if constexpr (lo < hi) chain_issue<lo>(c, lw);
  if constexpr (lo + 1 < hi) chain_issue<lo + 1>(c, lw);
  static_assert(hi - lo <= 2, "");
  if constexpr (G + 1 < NCH) chain_load_step<G + 1>(c, lw);
}
constexpr int SQWIN = 10, W3WIN = 10, W4WIN = 5;
constexpr int W3SG = 6, W3EP = 19, W4LP = 3, W4LS = 5;
template <int KS, int NR, bool SHIFT> struct ASrc {
  __amdgpu_buffer_rsrc_t rs;
  int soff, rstride, v16[SHIFT ? 4 : 1], v8[SHIFT ? 4 : 1];
  DI u32x4 load(int s, int r) const {
    const int o = soff + r * rstride + s * 1024;
    if (s < (KS == 20 ? 16 : 48)) return __builtin_amdgcn_raw_buffer_load_b128(rs, v16[SHIFT ? s >> 2 : 0], o, 16);
    const int v = v8[SHIFT ? s - 16 : 0];
    const u32x2 lo = __builtin_amdgcn_raw_buffer_load_b64(rs, v, o, 16), hi = __builtin_amdgcn_raw_buffer_load_b64(rs, v, o + 512, 16);
    return u32x4{lo[0], lo[1], hi[0], hi[1]};
  }
};
struct RingBase { LAS const char* p[3]; };
DI RingBase ring_base(LAS const char* p) {
  RingBase r{{p, p + 65536, p + 131072}};
  PIN(r.p[0]); PIN(r.p[1]); PIN(r.p[2]);
  return r;
}
template <int G0> DI u32x4 bfrag(const RingBase& rb, int i) {
  const int o = slot_of(G0 + i / CHF) * SLOT + (i % CHF) * 1024;
  return *(LAS const u32x4*)(rb.p[o >> 16] + (o & 65535));
}
template <int NR, int NU, int NJ, int GR, int NBUF, int G0, int WIN, int STRIDE, int S0, int I0, class START, class POST>
DI void gemm_core(f32x4 (&acc)[NR][NU], u32x4 (&af)[WIN][NR], const RingBase& rb, START start, POST post) {
  constexpr int GPS = (NU + GR - 1) / GR, NG = NJ * GPS, AH = NBUF - 1;
  u32x4 bf[NBUF][GR];
  auto gsz = [](int gi) { return (gi % GPS + 1) * GR <= NU ? GR : NU - (gi % GPS) * GR; };
  auto lin = [](int gi, int k) { return I0 + ((gi / GPS) * STRIDE + S0) * NU + (gi % GPS) * GR + k; };
  auto bread1 = [&](int gi, int k) {
    const int i = lin(gi, k);
    const int ip = k > 0 ? i - 1 : gi > 0 ? lin(gi - 1, (gi % GPS ? GR : NU - (GPS - 1) * GR) - 1) : (I0 > 0 ? I0 - 1 : i);
    if (i / CHF != ip / CHF) RAWBAR();
    bf[gi % NBUF][k] = bfrag<G0>(rb, i);
  };
#pragma unroll
  for (int gi = 0; gi < AH; ++gi)
#pragma unroll
    for (int k = 0; k < GR; ++k) if (k < gsz(gi)) bread1(gi, k);
  start();
  SB();
#pragma unroll
  for (int gi = 0; gi < NG; ++gi) {
    const int j = gi / GPS, u0 = (gi % GPS) * GR, n = gsz(gi);
#pragma unroll
    for (int k = GR - 1; k >= 0; --k) {
      if (k < n) { acc[0][u0 + k] = mfma16(bf[gi % NBUF][k], af[j % WIN][0], acc[0][u0 + k]); SB(); }
      if (gi + AH < NG && GR - 1 - k < gsz(gi + AH)) { bread1(gi + AH, GR - 1 - k); SB(); }
      if (NR == 2 && k < n) { acc[NR - 1][u0 + k] = mfma16(bf[gi % NBUF][k], af[j % WIN][NR - 1], acc[NR - 1][u0 + k]); SB(); }
    }
    if (u0 + n == NU) { post(j); SB(); }
  }
}
template <int NU, int KS, int GR, int NBUF, int G0, int WIN, class A, class PRE, class MID>
DI void gemm_phase(f32x4 (&acc)[1][NU], const A& asrc, const RingBase& rb, PRE pre, MID mid) {
  u32x4 af[WIN][1];
  gemm_core<1, NU, KS, GR, NBUF, G0, WIN, 1, 0, 0>(acc, af, rb,
    [&] {
      pre();
#pragma unroll
      for (int j = 0; j < WIN; ++j) af[j][0] = asrc.load(j, 0);
      SB();
      mid();
    },
    [&](int j) { if (j + WIN < KS) af[j % WIN][0] = asrc.load(j + WIN, 0); });
}

DI float quad_sum(float v) {
  auto a = __builtin_amdgcn_permlane16_swap(__float_as_uint(v), __float_as_uint(v), false, false);
  v = __uint_as_float(a[0]) + __uint_as_float(a[1]);
  auto b = __builtin_amdgcn_permlane32_swap(__float_as_uint(v), __float_as_uint(v), false, false);
  return __uint_as_float(b[0]) + __uint_as_float(b[1]);
}
DI void st_sc1(__amdgpu_buffer_rsrc_t rs, int voff, int soff, u32x4 v) { __builtin_amdgcn_raw_buffer_store_b128(v, rs, voff + soff, 0, 16); }
DI void st_sc1(__amdgpu_buffer_rsrc_t rs, int voff, int soff, u32x2 v) { __builtin_amdgcn_raw_buffer_store_b64(v, rs, voff + soff, 0, 16); }
DI void store5(__amdgpu_buffer_rsrc_t rs, int base, int cb, int l, const f32x4 (&v)[5]) {
  st_sc1(rs, l * 16, base + cb * 2048, u32x4{pk2(v[0][0], v[0][1]), pk2(v[0][2], v[0][3]), pk2(v[1][0], v[1][1]), pk2(v[1][2], v[1][3])});
  st_sc1(rs, l * 16, base + cb * 2048 + 1024, u32x4{pk2(v[2][0], v[2][1]), pk2(v[2][2], v[2][3]), pk2(v[3][0], v[3][1]), pk2(v[3][2], v[3][3])});
  st_sc1(rs, l * 8, base + 16384 + cb * 512, u32x2{pk2(v[4][0], v[4][1]), pk2(v[4][2], v[4][3])});
}
DI void put_stats(__amdgpu_buffer_rsrc_t rs, int off, const f32x4 (&v)[5], int l) {
  f32x4 a = v[0], b = v[0] * v[0];
#pragma unroll
  for (int t = 1; t < 5; ++t) { a += v[t]; b += v[t] * v[t]; }
  const float sa = quad_sum((a[0] + a[1]) + (a[2] + a[3])), sb = quad_sum((b[0] + b[1]) + (b[2] + b[3]));
  if (l < 16) st_sc1(rs, l * 8, off, u32x2{__float_as_uint(sa), __float_as_uint(sb)});
}
struct StatIn { u32x2 p, q; };
DI StatIn load_stats(__amdgpu_buffer_rsrc_t rs, int off, int l) {
  const int v = (l >> 4) * 256 + (l & 15) * 8;
  return StatIn{__builtin_amdgcn_raw_buffer_load_b64(rs, v, off, 16), __builtin_amdgcn_raw_buffer_load_b64(rs, v, off + 128, 16)};
}
DI void get_stats(const StatIn& s, float& mean, float& var) {
  const float a = quad_sum(__uint_as_float(s.p[0]) + __uint_as_float(s.q[0])), b = quad_sum(__uint_as_float(s.p[1]) + __uint_as_float(s.q[1]));
  mean = a * (1.f / D);
  var = fmaxf(b * (1.f / D) - mean * mean, 0.f);
}
DI float gelu_r(float a, float k1, float k2, float ri) {
  return a * __builtin_amdgcn_rcpf(fmaf(EX2(a * fmaf(a * a, k2, k1)), ri, ri));
}

template <int KQ>
DI void w4_part(char* ws, char* out, __amdgpu_buffer_rsrc_t rs, const RingBase& rb, LAS char* lds, int rp, int cb, int l, const f32x4 (&hv)[5],
                int late_cnt) {
  constexpr int OR = KQ & 1, OH = KQ >> 1, NJ = KQ < 2 ? 14 : 13;
  f32x4 acc[2][5];
  u32x4 af[W4WIN][2];
  ASrc<54, 2, false> as{rs, GIMG_OFF + rp * 55296, 55296, {l * 16}, {l * 8}};
  auto aload = [&](int j) { af[j % W4WIN][0] = as.load(4 * j + KQ, 0); af[j % W4WIN][1] = as.load(4 * j + KQ, 1); };
  asm volatile("" ::: "memory");
#pragma unroll
  for (int j = 0; j < W4WIN; ++j) aload(j);
  SB();
  waitv<2 * W4WIN>();
  if (l == 0) __hip_atomic_fetch_add((GAS unsigned*)(ws + late_cnt), 1u, __ATOMIC_RELAXED, __HIP_MEMORY_SCOPE_AGENT);
  RAWBAR();
  unsigned late;
  gemm_core<2, 5, NJ, 5, 2, 17, W4WIN, 4, KQ, 0>(acc, af, rb,
    [&] {
#pragma unroll
      for (int t = 0; t < 5; ++t) { acc[OR][t] = KQ < 2 ? hv[t] : f32x4{}; acc[1 - OR][t] = f32x4{}; }
    },
    [&](int j) {
      if (j == W4LP) late = __hip_atomic_load((GAS unsigned*)(ws + CNT_OFF + (CNT_L + rp + (l & 1)) * 128), __ATOMIC_RELAXED, __HIP_MEMORY_SCOPE_AGENT);
      if (j == W4LS) {
        if (!__all(late >= 16u)) wait_ge(ws, CNT_OFF + (CNT_L + rp + (l & 1)) * 128, 16);
        asm volatile("" ::: "memory");
      }
      if (j < W4LS) { if (j + W4WIN < 8) aload(j + W4WIN); }
      else if (j == W4LS) {
#pragma unroll
        for (int jj = 8; jj <= j + W4WIN; ++jj) if (jj < NJ) aload(jj);
      } else if (j + W4WIN < NJ) aload(j + W4WIN);
    });
  LAS char* xb = lds + slot_of(22) * SLOT + (rp & 2) * 15360 + l * 16;
#pragma unroll
  for (int r = 0; r < 2; ++r)
#pragma unroll
    for (int t = 0; t < 5; ++t) {
      const int own = r + ((t == 2 || t == 3) ? 2 : 0);
      if (own != KQ) *(LAS f32x4*)(xb + ((r * 5 + t) * 3 + (KQ < own ? KQ : KQ - 1)) * 1024) = acc[r][t];
    }
  BAR();
  auto sum = [&](int t) {
    LAS const char* p = xb + (OR * 5 + t) * 3072;
    return (acc[OR][t] + *(LAS const f32x4*)p) + (*(LAS const f32x4*)(p + 1024) + *(LAS const f32x4*)(p + 2048));
  };
  const int q = l >> 4;
  char* op = out + ((rp + OR) * 16 + (l & 15)) * 1280 + cb * 160;
  const f32x4 va = sum(2 * OH), vb = sum(2 * OH + 1);
  auto r0 = __builtin_amdgcn_permlane16_swap(pk2(va[0], va[1]), pk2(vb[0], vb[1]), false, false);
  auto r1 = __builtin_amdgcn_permlane16_swap(pk2(va[2], va[3]), pk2(vb[2], vb[3]), false, false);
  *(u32x4*)(op + OH * 64 + (q & 1) * 32 + (q >> 1) * 16) = u32x4{r0[0], r1[0], r0[1], r1[1]};
  if (OH == 0) { const f32x4 vc = sum(4); *(u32x2*)(op + 128 + q * 8) = u32x2{pk2(vc[0], vc[1]), pk2(vc[2], vc[3])}; }
}

DI void side_waves(char* ws, const WPtrs& wp, int sw, int head, int cb, int sl, LAS char* lds) {
  const unsigned lds0 = (unsigned)(uintptr_t)lds;
  const int l = threadIdx.x & 63, h = l >> 5, r32 = l & 31, u = head >> 1, e = head & 1;
  auto rs = __builtin_amdgcn_make_buffer_rsrc((void*)ws, 0, WS_END, 0x00020000);
  const int pa = (5 * u + 3 * e) * 512, pm = (5 * u + 2) * 512;
  auto vsrc = [&](int pc) {
    const int row = (pc * 3121) >> 16, d = pc - row * 21;
    return row * V_ROW + (d < 20 ? head * 20 + d : 640) * 16;
  };
  const int pc2 = sw < 2 ? (sw + 8) * 64 : 672 - 64;
  const int vA = l * 16, vB = h * 40960 + r32 * 16;
  {
    dma16(rs, lds0 + sw * 1024, vA, KIMG_OFF + sw * 40960 + pa);
    dma16(rs, lds0 + 8192 + sw * 1024, vB, KIMG_OFF + 2 * sw * 40960 + pm);
    dma16(rs, lds0 + (sw + 4) * 1024, vA, KIMG_OFF + (sw + 4) * 40960 + pa);
    SB();
  }
  const int vC0 = vsrc(sw * 64 + l), vC1 = vsrc((sw + 4) * 64 + l), vC2 = vsrc(pc2 + l);
  auto issue = [&](int c) {
    const unsigned dst = lds0 + (c & 3) * KV_CHUNK;
    const int kb = KIMG_OFF + c * 8 * 40960, vb = VIMG_OFF + c * 32 * V_ROW;
    dma16(rs, dst + sw * 1024, vA, kb + sw * 40960 + pa);
    dma16(rs, dst + 8192 + sw * 1024, vB, kb + 2 * sw * 40960 + pm);
    dma16(rs, dst + 12288 + sw * 1024, vC0, vb);
    dma16(rs, dst + (sw + 4) * 1024, vA, kb + (sw + 4) * 40960 + pa);
    dma16(rs, dst + 12288 + (sw + 4) * 1024, vC1, vb);
    dma16(rs, dst + 12288 + pc2 * 16, vC2, vb);
  };
  dma16(rs, lds0 + 12288 + sw * 1024, vC0, VIMG_OFF);
  dma16(rs, lds0 + 12288 + (sw + 4) * 1024, vC1, VIMG_OFF);
  dma16(rs, lds0 + 12288 + pc2 * 16, vC2, VIMG_OFF);
  issue(1);
  const int n2 = (l & 15) * 2, q = l >> 4;
  const int vk0 = 4 * q * 1280 + n2, vk1 = 4 * q * 3456 + n2, vk2 = ((q >> 1) * 20 + 4 * (q & 1)) * 1280 + n2, vk3 = q * 20 * 1280 + n2;
  unsigned w[2][8];
  int dst[2] = {0, 0}, cs[2] = {0, 0}, dup[2] = {0, 0};
  auto gather = [&](int f, int k) {
    const Job J = retile_job(wp, f);
    auto rw = __builtin_amdgcn_make_buffer_rsrc((void*)J.src, 0, J.bytes, 0x00020000);
    const int v = J.kind == 0 ? vk0 : J.kind == 1 ? vk1 : J.kind == 2 ? vk2 : vk3;
#pragma unroll
    for (int jj = 0; jj < 4; ++jj) {
      w[k][jj] = (unsigned short)__builtin_amdgcn_raw_buffer_load_b16(rw, v, J.o0 + jj * J.pitch, 0);
      w[k][4 + jj] = (unsigned short)__builtin_amdgcn_raw_buffer_load_b16(rw, v, J.o1 + jj * J.pitch, 0);
    }
    dst[k] = J.dst; cs[k] = J.cs; dup[k] = J.dup;
  };
  waitv<6>();
  RAWBAR();
  const unsigned selv = (l & 15) == 0 ? 0x3f803f80u : 0u;
  const u32x4 sel = {selv, selv, selv, selv};
  __builtin_amdgcn_s_setprio(3);
  LoaderCtx c;
  c.rs = rs; c.lds0 = lds0; c.sq = WT_OFF + cb * 102400; c.s3 = WT3_OFF + sl * 143360; c.s4 = WT4_OFF + cb * 276480; c.vl = l * 16;
#pragma unroll 1
  for (int i = 0; i < 8; ++i) {
    waitv<0>();
    if (i == 5 && l == 0)
      __hip_atomic_fetch_add((GAS unsigned*)(ws + CNT_OFF + (CNT_W + (blockIdx.x & 31)) * 128), 1u, __ATOMIC_RELAXED, __HIP_MEMORY_SCOPE_AGENT);
#pragma unroll
    for (int k = 0; k < 2; ++k) {
      if (dst[k]) {
#pragma unroll
        for (int jj = 0; jj < 8; ++jj) PIN(w[k][jj]);
        const u32x4 fr = {w[k][0] | w[k][1] << 16, w[k][2] | w[k][3] << 16, w[k][4] | w[k][5] << 16, w[k][6] | w[k][7] << 16};
        __builtin_amdgcn_raw_buffer_store_b128(fr, rs, dst[k] + l * 16, 0, 16);
        if (dup[k]) __builtin_amdgcn_raw_buffer_store_b128(fr, rs, dst[k] + 1024 + l * 16, 0, 16);
        if (cs[k]) {
          const f32x4 cv = mfma16(sel, fr, f32x4{});
          if (l < 16) __builtin_amdgcn_raw_buffer_store_b32(__float_as_uint(cv[0]), rs, cs[k] + l * 4, 0, 16);
        }
        dst[k] = 0;
      }
    }
    if (i < 6) issue(i + 2);
    if (i == 5) {
      const char *c0, *c1;
      CODE_ADDR(c0, "fused_kernel"); CODE_ADDR(c1, "code_end");
      const unsigned pf = code_touch(c0, c1, (blockIdx.x >> 3) * 4 + sw, 128);
      asm volatile("" :: "v"(pf));
    }
    if (sw == 0 && i == 6) wait_ge(ws, CNT_OFF + (CNT_W + (l & 31)) * 128, 32u);
    if (i == 7) {
      chain_issue<0>(c, sw); chain_issue<1>(c, sw);
    }
#pragma unroll
    for (int k = 0; k < 2; ++k) {
      const int f = blockIdx.x * 4 + sw + 1024 * (2 * i + k);
      if (f < 6720) gather(f, k);
    }
    RAWBAR();
  }
  chain_issue<2>(c, sw);
  chain_colsum(rs, (LAS float*)(lds + CSL_OFF), cb, sl);
  BAR();
  chain_load_step<0>(c, sw);
}

struct FusedArgs { const char* x; char* ws; char* out; WPtrs wp; };
extern "C" __global__ void __launch_bounds__(768, 3) fused_kernel(FusedArgs args) {
  __shared__ __attribute__((aligned(16))) char lds_[163840];
  LAS char* lds = (LAS char*)lds_;
  char* const ws = args.ws;
  const char* const x = args.x;
  char* const out = args.out;
  const int tid = threadIdx.x, l = tid & 63, wv = __builtin_amdgcn_readfirstlane(tid >> 6);
  const int xc = blockIdx.x & 7, j = blockIdx.x >> 3;
  const int head = xc * 4 + (j & 3), qb = j >> 2;
  const int grp = (xc >> 1) * 8 + (j & 7);
  const int cb = (xc & 1) * 4 + (j >> 3);
  const int sl = cb * 2 + (grp & 1);
  if (wv >= 8) {
    side_waves(ws, args.wp, wv - 8, head, cb, sl, lds);
    return;
  }
  {
    const int h = l >> 5, r32 = l & 31, hf = wv >> 2, wq = wv & 3, u = head >> 1, e = head & 1;
  const int pa = (5 * u + 3 * e) * 512, pm = (5 * u + 2) * 512;
  const int qt = qb * 8 + wq * 2;
  const char* qp = ws + QIMG_OFF + qt * 40960;
  const u32x4 qa0 = *(const u32x4*)(qp + pa + l * 16), qb0 = *(const u32x4*)(qp + 40960 + pa + l * 16);
  u32x2 qa1 = *(const u32x2*)(qp + pm + r32 * 16 + e * 8), qb1 = *(const u32x2*)(qp + 40960 + pm + r32 * 16 + e * 8);
  if (tid < CZ_B / 16) *(LAS u32x4*)(lds + CZ_OFF + tid * 16) = u32x4{0u, 0u, 0u, 0u};
  else if (tid < (CZ_B + CK_B) / 16) *(LAS u32x4*)(lds + CZ_OFF + tid * 16) = u32x4{0x3f80u, 0u, 0x3f80u, 0u};
  if (h) { qa1 = u32x2{0u, 0u}; qb1 = u32x2{0u, 0u}; }
  const int ka = l * 16 + hf * 4096, kb = 8192 + r32 * 16 + e * 8 + hf * 2048;
  const int va = 12288 + h * 336 + r32 * 16 + hf * 4 * V_TILE;
  const bool zl = r32 > 20;
  const int vz = CZ_OFF + h * 336 + (r32 < 28 ? 5 : 12) * 16;
  BAR();
  {
    const u32x4 a0 = *(LAS const u32x4*)(lds + l * 16);
    const u32x2 a1 = *(LAS const u32x2*)(lds + kb - hf * 2048);
    f32x16 s = mfma32k8(a1, qa1, f32x16{}), t = mfma32k8(a1, qb1, f32x16{});
    s = mfma32(a0, qa0, s); t = mfma32(a0, qb0, t);
    float m = max16(s), n = max16(t);
    m = fmaxf(m, swap32_other(m)); n = fmaxf(n, swap32_other(n));
    if (h) { qa1[0] = pk2(-m, 0.f); qb1[0] = pk2(-n, 0.f); }
  }
  u32x4 k0 = *(LAS const u32x4*)(lds + ka);
  u32x2 k1 = *(LAS const u32x2*)(lds + (h ? CK_OFF : kb));
  f32x16 sa0 = mfma32k8(k1, qa1, f32x16{}), sb0 = mfma32k8(k1, qb1, f32x16{}), sa1, sb1;
  sa0 = mfma32(k0, qa0, sa0); sb0 = mfma32(k0, qb0, sb0);
  k0 = *(LAS const u32x4*)(lds + 1024 + ka);
  k1 = *(LAS const u32x2*)(lds + (h ? CK_OFF : kb + 512));
  u32x4 v0 = *(LAS const u32x4*)(lds + (zl ? vz : va)), v1 = u32x4{0u, 0u, 0u, 0u};
  f32x16 oa = f32x16{}, ob = f32x16{};
  u32x4 pa0, pa1, pb0, pb1 = u32x4{0u, 0u, 0u, 0u}, k0n;
  u32x2 k1n;
#define EC(SC, P, i) do { float e0 = EX2(SC[4 * (i)]), e1 = EX2(SC[4 * (i) + 1]), e2 = EX2(SC[4 * (i) + 2]), e3 = EX2(SC[4 * (i) + 3]); \
    P[2 * ((i) & 1)] = pk2(e0, e1); P[2 * ((i) & 1) + 1] = pk2(e2, e3); PIN(P); SB(); } while (0)
#define TILE(AC, AN, BC, BN, K0, K1, K0N, K1N, t) do { \
    LAS const char* kp = (t) + 2 < 4 ? cur + ((t) + 2) * 1024 : nxt + ((t) - 2) * 1024; \
    LAS const char* kq = (t) + 2 < 4 ? kcur + ((t) + 2) * 512 : knxt + ((t) - 2) * 512; \
    LAS const char* vp = (t) + 1 < 4 ? vcur + ((t) + 1) * V_TILE : vnxt; \
    __builtin_amdgcn_s_setprio(3 - (((t) + 2) & 3));     \
    AN = mfma32k8(K1, qa1, f32x16{}); SB(); EC(AC, pa0, 0); \
    AN = mfma32(K0, qa0, AN); SB(); EC(AC, pa0, 1); \
    ob = mfma32(v1, pb1, ob); SB(); \
    v1 = *(LAS const u32x4*)(vcur + (t) * V_TILE + 672); SB(); EC(AC, pa1, 2); \
    oa = mfma32(v0, pa0, oa); SB(); \
    K0N = *(LAS const u32x4*)(kp + ka); K1N = *(LAS const u32x2*)(kq); SB(); EC(AC, pa1, 3); \
    BN = mfma32k8(K1, qb1, f32x16{}); SB(); EC(BC, pb0, 0); \
    BN = mfma32(K0, qb0, BN); SB(); EC(BC, pb0, 1); \
    oa = mfma32(v1, pa1, oa); SB(); EC(BC, pb1, 2); \
    ob = mfma32(v0, pb0, ob); SB(); \
    v0 = *(LAS const u32x4*)(vp); SB(); EC(BC, pb1, 3); \
  } while (0)
#pragma unroll 1
  for (int c = 0; c < 8; ++c) {
    LAS const char* cur = lds + (c & 3) * KV_CHUNK;
    LAS const char* nxt = lds + ((c + 1) & 3) * KV_CHUNK;
    LAS const char* vcur = zl ? lds + vz : cur + va;
    LAS const char* vnxt = zl ? lds + vz : nxt + va;
    LAS const char* kcur = h ? lds + CK_OFF : cur + kb;
    LAS const char* knxt = h ? lds + CK_OFF : nxt + kb;
    TILE(sa0, sa1, sb0, sb1, k0, k1, k0n, k1n, 0); TILE(sa1, sa0, sb1, sb0, k0n, k1n, k0, k1, 1);
    BAR();
    SB();
    TILE(sa0, sa1, sb0, sb1, k0, k1, k0n, k1n, 2); TILE(sa1, sa0, sb1, sb0, k0n, k1n, k0, k1, 3);
  }
  ob = mfma32(v1, pb1, ob);
  __builtin_amdgcn_s_setprio(0);

  {
    LAS char* mp = lds + MERGE_OFF + (wv * 64 + l) * 16;
    LAS const char* mq = lds + MERGE_OFF + ((wv ^ 4) * 64 + l) * 16;
#pragma unroll
    for (int g = 0; g < 3; ++g)
      *(LAS f32x4*)(mp + g * 8192) = hf ? f32x4{oa[4 * g], oa[4 * g + 1], oa[4 * g + 2], oa[4 * g + 3]} : f32x4{ob[4 * g], ob[4 * g + 1], ob[4 * g + 2], ob[4 * g + 3]};
    BAR();
    f32x16 o = hf ? ob : oa;
#pragma unroll
    for (int g = 0; g < 3; ++g) {
      const f32x4 t = *(LAS const f32x4*)(mq + g * 8192);
#pragma unroll
      for (int i = 0; i < 4; ++i) o[4 * g + i] += t[i];
    }
    attn_finish(ws, o, hf ? qb0 : qa0, hf ? qb1 : qa1, qt + hf, head, l);
  }
  }
  auto rs = __builtin_amdgcn_make_buffer_rsrc((void*)ws, 0, WS_END, 0x00020000);
  const int m = l & 15, q = l >> 4, rt = grp * 4 + (wv & 3), row = rt * 16 + m;
  LAS float* csl = (LAS float*)(lds + CSL_OFF);
  const RingBase rb = ring_base(lds + l * 16);
  f32x4 acc[1][5], hv[5];
  StatIn si[2];
  const int tst = (rt * 8 + cb) * 128;
  if (wv < 4) {
  BAR();
  {
    u32x2 xr[5];
    ASrc<20, 1, false> as{rs, OIMG_OFF + rt * 20480, 0, {l * 16}, {l * 8}};
    gemm_phase<5, 20, 5, 3, 0, SQWIN>(acc, as, rb,
      [&] { wait_ge(ws, CNT_OFF + (CNT_O + rt) * 128, 32); },
      [&] {
#pragma unroll
        for (int t = 0; t < 5; ++t) { xr[t] = *(const u32x2*)(x + row * 1280 + (cb * 80 + t * 16 + 4 * q) * 2); acc[0][t] = f32x4{}; }
      });
#pragma unroll
    for (int t = 0; t < 5; ++t)
      hv[t] = acc[0][t] + f32x4{__uint_as_float(xr[t][0] << 16), __uint_as_float(xr[t][0] & 0xffff0000u), __uint_as_float(xr[t][1] << 16), __uint_as_float(xr[t][1] & 0xffff0000u)};
  }
  store5(rs, HIMG_OFF + rt * 20480, cb, l, hv);
  put_stats(rs, ST_OFF + tst, hv, l);
  signal(ws, CNT_OFF + rt * 128, l == 0);
  RAWBAR();
  float mean1, r1, r12;
  {
    ASrc<20, 1, false> as{rs, HIMG_OFF + rt * 20480, 0, {l * 16}, {l * 8}};
    gemm_phase<5, 20, 5, 3, 4, SQWIN>(acc, as, rb,
      [&] { wait_ge(ws, CNT_OFF + rt * 128, 8); si[0] = load_stats(rs, ST_OFF + rt * 1024, l); },
      [&] {
        float var;
        get_stats(si[0], mean1, var);
        r1 = __builtin_amdgcn_rsqf(var + 1e-5f);
        r12 = r1 * __builtin_amdgcn_rsqf(var * r1 * r1 + 1e-5f);
#pragma unroll
        for (int t = 0; t < 5; ++t) acc[0][t] = *(LAS const f32x4*)(csl + t * 16 + 4 * q) * -mean1;
      });
#pragma unroll
    for (int t = 0; t < 5; ++t)
#pragma unroll
      for (int i = 0; i < 4; ++i) acc[0][t][i] = gelu_r(acc[0][t][i], -2.3022082f * r12, -0.10294324f * r12 * r12 * r12, __builtin_amdgcn_rcpf(r12));
    store5(rs, MIMG_OFF + rt * 20480, cb, l, acc[0]);
    signal(ws, CNT_OFF + (128 + ((rt + (l & 1) * ((cb & 2) ? -1 : 1)) & 127)) * 128, l < 2);
  }
  RAWBAR();
  {
    ASrc<20, 1, true> as;
    as.rs = __builtin_amdgcn_make_buffer_rsrc((void*)(ws + MIMG_OFF), 0, OIMG_B, 0x00020000);
    as.soff = 0; as.rstride = 0;
#pragma unroll
    for (int cg = 0; cg < 4; ++cg) {
      const int sr = row + (cg & 1 ? 1 : -1) * (1 + (cg >> 1));
      const bool in = (unsigned)sr < (unsigned)S;
      as.v16[cg] = in ? (sr >> 4) * 20480 + q * 256 + (sr & 15) * 16 : 0x70000000;
      as.v8[cg] = in ? (sr >> 4) * 20480 + q * 128 + (sr & 15) * 8 : 0x70000000;
    }
    gemm_phase<5, 20, 5, 3, 8, SQWIN>(acc, as, rb,
      [&] { wait_ge(ws, CNT_OFF + (128 + rt) * 128, 16); },
      [&] {
#pragma unroll
        for (int t = 0; t < 5; ++t) acc[0][t] = (hv[t] - mean1) * r1;
      });
  }
  store5(rs, XIMG_OFF + rt * 20480, cb, l, acc[0]);
  put_stats(rs, ST_OFF + ST_B + tst, acc[0], l);
  signal(ws, CNT_OFF + (256 + rt) * 128, l == 0);
#pragma unroll
  for (int t = 0; t < 5; ++t) hv[t] += acc[0][t];
  } else {
    BAR();
#pragma unroll
    for (int i = 1; i < 12; ++i) RAWBAR();
#pragma unroll
    for (int t = 0; t < 5; ++t) hv[t] = f32x4{};
  }
  RAWBAR();
  {
    const int r3t = (grp & ~1) * 4 + wv, rp = grp * 4 + (wv & 2), kq = (wv & 1) + 2 * (wv >> 2);
    ASrc<20, 1, false> as{rs, XIMG_OFF + r3t * 20480, 0, {l * 16}, {l * 8}};
    u32x4 af[20][1];
    f32x4 a1[1][4], a2[1][3];
    float k1, k2, ri, nmean;
    const int gb = GIMG_OFF + r3t * 55296;
    gemm_core<1, 4, 20, 4, 3, 12, 20, 1, 0, 0>(a1, af, rb,
      [&] {
        wait_ge(ws, CNT_OFF + (256 + r3t) * 128, 8);
        si[0] = load_stats(rs, ST_OFF + ST_B + r3t * 1024, l);
#pragma unroll
        for (int j = 0; j < W3WIN; ++j) af[j][0] = as.load(j, 0);
        SB();
        float mean, var;
        get_stats(si[0], mean, var);
        const float r3 = __builtin_amdgcn_rsqf(var + 1e-5f);
        k1 = -2.3022082f * r3; k2 = -0.10294324f * r3 * r3 * r3; ri = (var + 1e-5f) * r3; nmean = -mean;
#pragma unroll
        for (int t = 0; t < 4; ++t) a1[0][t] = *(LAS const f32x4*)(csl + 80 + t * 16 + 4 * q) * nmean;
      },
      [&](int j) { if (j + W3WIN < 20) af[j + W3WIN][0] = as.load(j + W3WIN, 0); });
    unsigned early;
    gemm_core<1, 3, 20, 3, 3, 12, 20, 1, 0, 80>(a2, af, rb,
      [&] {
#pragma unroll
        for (int tp = 0; tp < 2; ++tp) {
          float g[8];
#pragma unroll
          for (int i = 0; i < 8; ++i) g[i] = gelu_r(a1[0][2 * tp + (i >> 2)][i & 3], k1, k2, ri);
          st_sc1(rs, l * 16, gb + (sl * 2 + tp) * 1024, u32x4{pk2(g[0], g[1]), pk2(g[2], g[3]), pk2(g[4], g[5]), pk2(g[6], g[7])});
        }
#pragma unroll
        for (int t = 0; t < 3; ++t) a2[0][t] = *(LAS const f32x4*)(csl + 144 + t * 16 + 4 * q) * nmean;
      },
      [&](int j) {
        if (j == W3SG) signal(ws, CNT_OFF + (384 + r3t) * 128, l == 0);
        if (j == W3EP) early = __hip_atomic_load((GAS unsigned*)(ws + CNT_OFF + (384 + rp + (l & 1)) * 128), __ATOMIC_RELAXED, __HIP_MEMORY_SCOPE_AGENT);
      });
    {
      float g[12];
#pragma unroll
      for (int i = 0; i < 12; ++i) g[i] = gelu_r(a2[0][i >> 2][i & 3], k1, k2, ri);
      st_sc1(rs, l * 16, gb + (32 + sl) * 1024, u32x4{pk2(g[0], g[1]), pk2(g[2], g[3]), pk2(g[4], g[5]), pk2(g[6], g[7])});
      if (sl < 12) st_sc1(rs, l * 8, gb + 49152 + sl * 512, u32x2{pk2(g[8], g[9]), pk2(g[10], g[11])});
      SB();
      if (!__all(early >= 16u)) wait_ge(ws, CNT_OFF + (384 + rp + (l & 1)) * 128, 16);
    }
    if (kq == 0) w4_part<0>(ws, out, rs, rb, lds, rp, cb, l, hv, CNT_OFF + (CNT_L + r3t) * 128);
    else if (kq == 1) w4_part<1>(ws, out, rs, rb, lds, rp, cb, l, hv, CNT_OFF + (CNT_L + r3t) * 128);
    else if (kq == 2) w4_part<2>(ws, out, rs, rb, lds, rp, cb, l, hv, CNT_OFF + (CNT_L + r3t) * 128);
    else w4_part<3>(ws, out, rs, rb, lds, rp, cb, l, hv, CNT_OFF + (CNT_L + r3t) * 128);
  }
}

extern "C" __device__ __attribute__((noinline, used)) void code_end() { asm volatile("s_nop 0"); }

extern "C" void kernel_launch(void* const* d_in, const int* in_sizes, int n_in, void* d_out, int out_size, void* d_ws, size_t ws_size, hipStream_t st) {
  FusedArgs a;
  a.x = (const char*)d_in[0]; a.ws = (char*)d_ws; a.out = (char*)d_out;
  a.wp.wo = (const char*)d_in[2]; a.wp.w1 = (const char*)d_in[4]; a.wp.w2 = (const char*)d_in[5]; a.wp.w3 = (const char*)d_in[6]; a.wp.w4 = (const char*)d_in[7];
  QkvArgs qa;
  qa.x = a.x; qa.w = (const char*)d_in[1]; qa.ws = a.ws;
  qkv_kernel<<<256, 768, 0, st>>>(qa);
  fused_kernel<<<256, 768, 0, st>>>(a);
}
```
